```python
import torch

def _softmax(x, axis=-1):
    return torch.softmax(x, dim=axis)
def _gelu(x):
    return torch.nn.functional.gelu(x, approximate="tanh")
def _silu(x):
    return torch.nn.functional.silu(x)
def _sigmoid(x):
    return torch.sigmoid(x)
def _layernorm(x, axis=-1, eps=1e-5):
    m = x.mean(dim=axis, keepdim=True)
    v = x.var(dim=axis, keepdim=True, unbiased=False)
    return (x - m) / torch.sqrt(v + eps)
def _scatter_add(tgt, idx, src, axis=0):
    return tgt.clone().index_add_(axis, idx, src)
def _segment_sum(src, seg, n):
    out = torch.zeros((n,) + src.shape[1:], dtype=src.dtype)
    return out.index_add_(0, seg, src)
def _pad(x, pw):
    flat = tuple(v for pair in reversed(pw) for v in pair)
    return torch.nn.functional.pad(x, flat)

kernel_name = 'standalone__sliding_block_cached__decode'

def setup_inputs():

    _w = (lambda a: (lambda t: t.to(torch.bfloat16) if t.is_floating_point() else t)(torch.as_tensor(a)))
    B, Sq, D = 1, 1, 640
    Hq, Hkv, Sk, Dh = 32, 32, 32768, 192
    assert Hq % Hkv == 0, (Hq, Hkv)
    return {
        "x": _w((lambda *s: torch.randn(*s, dtype=torch.bfloat16) * 0.8)(B, Sq, D)),
        "k_cache": _w((lambda *s: torch.randn(*s, dtype=torch.bfloat16) * 0.8)(B, Hkv, Sk, Dh)),
        "v_cache": _w((lambda *s: torch.randn(*s, dtype=torch.bfloat16) * 0.05)(B, Hkv, Sk, Dh)),
        "wq": _w((lambda *s: torch.randn(*s, dtype=torch.bfloat16) * 0.8)(D, Hq * Dh) * (D ** -0.5)),
        "wo": _w((lambda *s: torch.randn(*s, dtype=torch.bfloat16) * 0.05)(Hq * Dh, D) * ((Hq * Dh) ** -0.5)),
    }

def reference(x, k_cache, v_cache, wq, wo):
    B, Sq, D = x.shape
    Hq, Dh = 32, 192
    Hkv, Sk = k_cache.shape[1], k_cache.shape[2]
    W = min(4096, max(256, Sk // 4))
    rep = Hq // Hkv
    q = (lambda x, *d: x.permute(*d))((x @ wq).reshape(B, Sq, Hq, Dh), 0, 2, 1, 3)
    k = (lambda x, n, axis: torch.repeat_interleave(x, n, dim=axis))(k_cache[:, :, Sk - W:, :], rep, axis=1)
    v = (lambda x, n, axis: torch.repeat_interleave(x, n, dim=axis))(v_cache[:, :, Sk - W:, :], rep, axis=1)
    scores = torch.einsum("bhqd,bhkd->bhqk", q, k) * (Dh ** -0.5)
    p = _softmax(scores, axis=-1)
    o = torch.einsum("bhqk,bhkd->bhqd", p, v)
    return (lambda x, *d: x.permute(*d))(o, 0, 2, 1, 3).reshape(B, Sq, Hq * Dh) @ wo
```

```cpp
#include <hip/hip_runtime.h>
#include <hip/hip_bf16.h>
#include <stdint.h>

typedef unsigned u32x4 __attribute__((ext_vector_type(4)));
typedef float f32x4 __attribute__((ext_vector_type(4)));
typedef __bf16 bf16x2 __attribute__((ext_vector_type(2)));

constexpr int D = 640, H = 32, SK = 32768, DH = 192, W = 4096, NQ = H * DH;
constexpr int NSPLIT = 8, KPS = W / NSPLIT;

__device__ __forceinline__ float bflo(unsigned u) { return __builtin_bit_cast(float, u << 16); }
__device__ __forceinline__ float bfhi(unsigned u) { return __builtin_bit_cast(float, u & 0xffff0000u); }
__device__ __forceinline__ float dot2(unsigned k, unsigned q, float acc) {
  return __builtin_amdgcn_fdot2_f32_bf16(__builtin_bit_cast(bf16x2, k), __builtin_bit_cast(bf16x2, q), acc, false);
}

__global__ void __launch_bounds__(256) k_q(const unsigned short* __restrict__ x, const unsigned short* __restrict__ wq, __hip_bfloat16* __restrict__ q) {
  __shared__ float red[32][64 + 1];
  const int tid = threadIdx.x, c8 = tid & 7, rg = tid >> 3;
  const int col0 = blockIdx.x * 64 + c8 * 8;
  float acc[8];
#pragma unroll
  for (int i = 0; i < 8; ++i) acc[i] = 0.f;
#pragma unroll
  for (int it = 0; it < 20; ++it) {
    const int r = it * 32 + rg;
    const u32x4 w = __builtin_nontemporal_load(reinterpret_cast<const u32x4*>(wq + (size_t)r * NQ + col0));
    const float xv = bflo(x[r]);
#pragma unroll
    for (int j = 0; j < 4; ++j) { acc[2 * j] = fmaf(xv, bflo(w[j]), acc[2 * j]); acc[2 * j + 1] = fmaf(xv, bfhi(w[j]), acc[2 * j + 1]); }
  }
#pragma unroll
  for (int i = 0; i < 8; ++i) red[rg][c8 * 8 + i] = acc[i];
  __syncthreads();
  if (tid < 64) {
    float s = 0.f;
    for (int r = 0; r < 32; ++r) s += red[r][tid];
    q[blockIdx.x * 64 + tid] = __float2bfloat16(s);
  }
}

__global__ void __launch_bounds__(512) k_attn(const unsigned short* __restrict__ q, const unsigned short* __restrict__ kc, const unsigned short* __restrict__ vc, float* __restrict__ part) {
  __shared__ float sc[KPS];
  __shared__ float red[64][DH + 4];
  __shared__ float wred[16];
  const int h = blockIdx.x >> 3, s = blockIdx.x & 7;
  const int tid = threadIdx.x, c = tid & 7, kg = tid >> 3;
  const size_t base = ((size_t)h * SK + (SK - W) + (size_t)s * KPS) * DH;
  u32x4 qv[3];
#pragma unroll
  for (int j = 0; j < 3; ++j) qv[j] = *reinterpret_cast<const u32x4*>(q + h * DH + j * 64 + c * 8);
  const float C2 = 0.07216878364870322f * 1.4426950408889634f;
  float mloc = -INFINITY;
  float myscore[8];
#pragma unroll
  for (int it = 0; it < 8; ++it) {
    const int key = it * 64 + kg;
    float a = 0.f;
#pragma unroll
    for (int j = 0; j < 3; ++j) {
      const u32x4 kv = __builtin_nontemporal_load(reinterpret_cast<const u32x4*>(kc + base + (size_t)key * DH + j * 64 + c * 8));
#pragma unroll
      for (int e = 0; e < 4; ++e) a = dot2(kv[e], qv[j][e], a);
    }
    a += __shfl_xor(a, 1); a += __shfl_xor(a, 2); a += __shfl_xor(a, 4);
    a *= C2;
    myscore[it] = a;
    mloc = fmaxf(mloc, a);
  }
  mloc = fmaxf(mloc, __shfl_xor(mloc, 8)); mloc = fmaxf(mloc, __shfl_xor(mloc, 16)); mloc = fmaxf(mloc, __shfl_xor(mloc, 32));
  if ((tid & 63) == 0) wred[tid >> 6] = mloc;
  __syncthreads();
  float m = wred[0];
#pragma unroll
  for (int i = 1; i < 8; ++i) m = fmaxf(m, wred[i]);
  float acc[24];
#pragma unroll
  for (int i = 0; i < 24; ++i) acc[i] = 0.f;
  float l = 0.f;
#pragma unroll
  for (int it = 0; it < 8; ++it) {
    const int key = it * 64 + kg;
    const float p = __builtin_amdgcn_exp2f(myscore[it] - m);
    l += p;
#pragma unroll
    for (int j = 0; j < 3; ++j) {
      const u32x4 vv = __builtin_nontemporal_load(reinterpret_cast<const u32x4*>(vc + base + (size_t)key * DH + j * 64 + c * 8));
#pragma unroll
      for (int e = 0; e < 4; ++e) { acc[j * 8 + 2 * e] = fmaf(p, bflo(vv[e]), acc[j * 8 + 2 * e]); acc[j * 8 + 2 * e + 1] = fmaf(p, bfhi(vv[e]), acc[j * 8 + 2 * e + 1]); }
    }
  }
#pragma unroll
  for (int j = 0; j < 3; ++j)
#pragma unroll
    for (int e = 0; e < 8; ++e) red[kg][j * 64 + c * 8 + e] = acc[j * 8 + e];
  if (c == 0) red[kg][DH] = l;
  __syncthreads();
  if (tid <= DH) {
    float t = 0.f;
    for (int r = 0; r < 64; ++r) t += red[r][tid];
    part[(size_t)blockIdx.x * 256 + tid] = t;
    if (tid == 0) part[(size_t)blockIdx.x * 256 + DH + 1] = m;
  }
}

__global__ void __launch_bounds__(128) k_o(const float* __restrict__ part, const unsigned short* __restrict__ wo, float* __restrict__ y) {
  __shared__ float o[24];
  const int h = blockIdx.x >> 3, s = blockIdx.x & 7, tid = threadIdx.x;
  if (tid < 24) {
    float M = -INFINITY;
    for (int i = 0; i < 8; ++i) M = fmaxf(M, part[(size_t)(h * 8 + i) * 256 + DH + 1]);
    float L = 0.f, a = 0.f;
    for (int i = 0; i < 8; ++i) {
      const float w = __builtin_amdgcn_exp2f(part[(size_t)(h * 8 + i) * 256 + DH + 1] - M);
      L += w * part[(size_t)(h * 8 + i) * 256 + DH];
      a += w * part[(size_t)(h * 8 + i) * 256 + s * 24 + tid];
    }
    o[tid] = __bfloat162float(__float2bfloat16(a / L));
  }
  __syncthreads();
  if (tid < 80) {
    float acc[8];
#pragma unroll
    for (int i = 0; i < 8; ++i) acc[i] = 0.f;
#pragma unroll
    for (int r = 0; r < 24; ++r) {
      const u32x4 w = __builtin_nontemporal_load(reinterpret_cast<const u32x4*>(wo + (size_t)(h * DH + s * 24 + r) * D + tid * 8));
      const float ov = o[r];
#pragma unroll
      for (int j = 0; j < 4; ++j) { acc[2 * j] = fmaf(ov, bflo(w[j]), acc[2 * j]); acc[2 * j + 1] = fmaf(ov, bfhi(w[j]), acc[2 * j + 1]); }
    }
#pragma unroll
    for (int i = 0; i < 8; ++i) y[(size_t)blockIdx.x * D + tid * 8 + i] = acc[i];
  }
}

__global__ void __launch_bounds__(256) k_fin(const float* __restrict__ y, __hip_bfloat16* __restrict__ out) {
  __shared__ float red[16][16];
  const int tid = threadIdx.x, c = tid & 15, g = tid >> 4;
  const int col = blockIdx.x * 16 + c;
  float a = 0.f;
#pragma unroll
  for (int i = 0; i < 16; ++i) a += y[(size_t)(g * 16 + i) * D + col];
  red[g][c] = a;
  __syncthreads();
  if (tid < 16) { float t = 0.f; for (int i = 0; i < 16; ++i) t += red[i][tid]; out[blockIdx.x * 16 + tid] = __float2bfloat16(t); }
}

extern "C" void kernel_launch(void* const* d_in, const int* in_sizes, int n_in, void* d_out, int out_size, void* d_ws, size_t ws_size, hipStream_t stream) {
  const unsigned short* x = (const unsigned short*)d_in[0];
  const unsigned short* kc = (const unsigned short*)d_in[1];
  const unsigned short* vc = (const unsigned short*)d_in[2];
  const unsigned short* wq = (const unsigned short*)d_in[3];
  const unsigned short* wo = (const unsigned short*)d_in[4];
  char* ws = (char*)d_ws;
  __hip_bfloat16* q = (__hip_bfloat16*)ws;
  float* part = (float*)(ws + 65536);
  float* y = (float*)(ws + 65536 + 262144);
  k_q<<<96, 256, 0, stream>>>(x, wq, q);
  k_attn<<<256, 512, 0, stream>>>((const unsigned short*)q, kc, vc, part);
  k_o<<<256, 128, 0, stream>>>(part, wo, y);
  k_fin<<<40, 256, 0, stream>>>(y, (__hip_bfloat16*)d_out);
}
```

```cpp
#include <hip/hip_runtime.h>
#include <hip/hip_bf16.h>
#include <stdint.h>

typedef unsigned u32x4 __attribute__((ext_vector_type(4)));
typedef float f32x4 __attribute__((ext_vector_type(4)));
typedef __bf16 bf16x2 __attribute__((ext_vector_type(2)));
typedef __bf16 bf16x8 __attribute__((ext_vector_type(8)));
typedef unsigned long long u64;
#define GAS __attribute__((address_space(1)))
typedef GAS u64 gu64;
#define RLX_AGENT __ATOMIC_RELAXED, __HIP_MEMORY_SCOPE_AGENT

constexpr int D = 640, H = 32, SK = 32768, DH = 192, W = 4096, NQ = H * DH;
#define WIN 3
#define VPRE WIN
#define VMID 2
#define WOEARLY 8
#define BARLATE 1
#define QSLEEP 16
#define PSLEEP 1
constexpr unsigned SPIN_CAP = 1u << 16;
constexpr size_t WS_EPOCH = 0;
constexpr size_t WS_QP = 4096;
constexpr size_t WS_OP = WS_QP + 256 * 192 * 8;
constexpr size_t WS_YP = WS_OP + 256 * 2048;
constexpr size_t WS_ST = 2u << 20;

__device__ __forceinline__ u32x4 ldnt(const void* p) { return __builtin_nontemporal_load(reinterpret_cast<const u32x4*>(p)); }
__device__ __forceinline__ float dot2(unsigned k, unsigned q, float acc) {
  return __builtin_amdgcn_fdot2_f32_bf16(__builtin_bit_cast(bf16x2, k), __builtin_bit_cast(bf16x2, q), acc, false);
}
__device__ __forceinline__ f32x4 mfma(u32x4 a, u32x4 b, f32x4 c) {
  return __builtin_amdgcn_mfma_f32_16x16x32_bf16(__builtin_bit_cast(bf16x8, a), __builtin_bit_cast(bf16x8, b), c, 0, 0, 0);
}
__device__ __forceinline__ unsigned pkbf(float lo, float hi) {
  bf16x2 v = {(__bf16)lo, (__bf16)hi};
  return __builtin_bit_cast(unsigned, v);
}
template <int CTRL> __device__ __forceinline__ float dpp(float x) {
  return __builtin_bit_cast(float, __builtin_amdgcn_mov_dpp(__builtin_bit_cast(int, x), CTRL, 0xf, 0xf, true));
}
constexpr int XOR1 = 0xB1, XOR2 = 0x4E, XOR7 = 0x141, XOR8 = 0x128, ROR4 = 0x124;
__device__ __forceinline__ float sum8(float x) { x += dpp<XOR1>(x); x += dpp<XOR2>(x); x += dpp<XOR7>(x); return x; }
__device__ __forceinline__ float max8(float x) { x = fmaxf(x, dpp<XOR1>(x)); x = fmaxf(x, dpp<XOR2>(x)); x = fmaxf(x, dpp<XOR7>(x)); return x; }
__device__ __forceinline__ float xrow_max(float x) {
  auto s = __builtin_amdgcn_permlane16_swap(__float_as_uint(x), __float_as_uint(x), false, false);
  x = fmaxf(__uint_as_float(s[0]), __uint_as_float(s[1]));
  auto t = __builtin_amdgcn_permlane32_swap(__float_as_uint(x), __float_as_uint(x), false, false);
  return fmaxf(__uint_as_float(t[0]), __uint_as_float(t[1]));
}
__device__ __forceinline__ float xrow_sum(float x) {
  auto s = __builtin_amdgcn_permlane16_swap(__float_as_uint(x), __float_as_uint(x), false, false);
  x = __uint_as_float(s[0]) + __uint_as_float(s[1]);
  auto t = __builtin_amdgcn_permlane32_swap(__float_as_uint(x), __float_as_uint(x), false, false);
  return __uint_as_float(t[0]) + __uint_as_float(t[1]);
}
__device__ __forceinline__ void put(gu64* g, unsigned tag, float v) {
  __hip_atomic_store(g, ((u64)tag << 32) | __float_as_uint(v), RLX_AGENT);
}
__device__ __forceinline__ void put2(__amdgpu_buffer_rsrc_t rs, int off, unsigned tag, float a, float b) {
  const u32x4 v = {__float_as_uint(a), tag, __float_as_uint(b), tag};
  __builtin_amdgcn_raw_buffer_store_b128(v, rs, off, 0, 16);
}
__device__ __forceinline__ u32x4 get2(__amdgpu_buffer_rsrc_t rs, int off) { return __builtin_amdgcn_raw_buffer_load_b128(rs, off, 0, 16); }
#define WAITVM(n) asm volatile("s_waitcnt vmcnt(%0)" ::"i"(n) : "memory")
#define BAR() do { asm volatile("s_waitcnt lgkmcnt(0)\n\ts_barrier" ::: "memory"); } while (0)
#define LAS __attribute__((address_space(3)))
#define MEMF() asm volatile("" ::: "memory")
#define TS(i)
#define TSOUT()

__global__ void __launch_bounds__(576, 3) k_fused(const unsigned short* __restrict__ x, const unsigned short* __restrict__ kc, const unsigned short* __restrict__ vc,
                                                  const unsigned short* __restrict__ wq, const unsigned short* __restrict__ wo, unsigned short* __restrict__ out, unsigned char* __restrict__ ws) {
  __shared__ __attribute__((aligned(16))) float lds[8 * 2 * 192 + 16 + 16 * 80 + 96 + 4];
  float* l_o = lds;
  float* l_ml = lds + 8 * 2 * 192;
  float* l_y = l_ml + 16;
  unsigned* l_q = (unsigned*)(l_y + 16 * 80);
  unsigned* l_tag = l_q + 96;
  TS(0);
  asm volatile("" ::"s"(x), "s"(kc), "s"(vc), "s"(wq), "s"(wo), "s"(out), "s"(ws));
  TS(12);
  const int b = blockIdx.x, h = (b & 7) * 4 + (b >> 6), s = (b >> 3) & 7, g = h * 8 + s;
  const int tid = threadIdx.x, lane = tid & 63, wv = __builtin_amdgcn_readfirstlane(tid >> 6);
  const int kg = lane >> 3, c = lane & 7;
  u32x4 am;
#pragma unroll
  for (int i = 0; i < 4; ++i) am[i] = (i == (c >> 1)) ? ((c & 1) ? 0xffff0000u : 0x0000ffffu) : 0u;

  const __amdgpu_buffer_rsrc_t rs = __builtin_amdgcn_make_buffer_rsrc((void*)ws, 0, (int)WS_ST, 0x00020000);
  if (__builtin_expect(wv == 0, 1)) {
    __builtin_amdgcn_s_setprio(3);
    const unsigned short* wp = wq + (size_t)(80 * s + 10 * kg) * NQ + h * DH + 8 * c;
    const unsigned* xp = reinterpret_cast<const unsigned*>(x + 80 * s + 10 * kg);
    unsigned xr[5];
#pragma unroll
    for (int i = 0; i < 5; ++i) xr[i] = xp[i];
    const unsigned tag = (unsigned)__hip_atomic_load((gu64*)(ws + WS_EPOCH), RLX_AGENT) + 1u;
    u32x4 wr[30];
#pragma unroll
    for (int n = 0; n < 10; ++n)
#pragma unroll
      for (int j = 0; j < 3; ++j) wr[n * 3 + j] = ldnt(wp + (size_t)n * NQ + 64 * j);
    if (lane == 0) l_tag[1] = 0u;
    __builtin_amdgcn_sched_barrier(0);
    BAR();
    __builtin_amdgcn_sched_barrier(0);
    TS(1);
    f32x4 acc[3] = {{0, 0, 0, 0}, {0, 0, 0, 0}, {0, 0, 0, 0}};
#pragma unroll
    for (int n = 0; n < 10; ++n) {
      const unsigned xv = (n & 1) ? (xr[n >> 1] >> 16) : (xr[n >> 1] & 0xffffu);
      const unsigned pk = xv | (xv << 16);
      const u32x4 a = {pk & am[0], pk & am[1], pk & am[2], pk & am[3]};
#pragma unroll
      for (int j = 0; j < 3; ++j) acc[j] = mfma(a, wr[n * 3 + j], acc[j]);
    }
    TS(2);
    if ((lane >> 5) == ((lane >> 3) & 1)) {
#pragma unroll
      for (int j = 0; j < 3; ++j) *reinterpret_cast<f32x4*>(l_y + (lane >> 5) * 192 + 64 * j + 8 * c + 4 * ((lane >> 4) & 1)) = acc[j];
    }
    asm volatile("s_waitcnt lgkmcnt(0)" ::: "memory");
    {
      const float2 a0 = *reinterpret_cast<const float2*>(l_y + 2 * lane), a1 = *reinterpret_cast<const float2*>(l_y + 192 + 2 * lane);
      put2(rs, (int)WS_QP + g * 1536 + lane * 16, tag, a0.x + a1.x, a0.y + a1.y);
      if (lane < 32) {
        const float2 b0 = *reinterpret_cast<const float2*>(l_y + 128 + 2 * lane), b1 = *reinterpret_cast<const float2*>(l_y + 192 + 128 + 2 * lane);
        put2(rs, (int)WS_QP + g * 1536 + 1024 + lane * 16, tag, b0.x + b1.x, b0.y + b1.y);
      }
    }
    TS(3);
    const int o1 = (int)WS_QP + h * 8 * 1536 + lane * 16;
    const int o2 = (int)WS_QP + (h * 8 + (lane >> 5) * 4) * 1536 + 1024 + (lane & 31) * 16;
    float q0, q1, q2, q3;
    __builtin_amdgcn_s_sleep(QSLEEP);
    for (unsigned spins = 0;; ++spins) {
      MEMF();
      u32x4 t[12];
#pragma unroll
      for (int i = 0; i < 8; ++i) t[i] = get2(rs, o1 + i * 1536);
#pragma unroll
      for (int i = 0; i < 4; ++i) t[8 + i] = get2(rs, o2 + i * 1536);
      bool ok = true;
      q0 = q1 = q2 = q3 = 0.f;
#pragma unroll
      for (int i = 0; i < 8; ++i) { ok &= t[i][1] == tag && t[i][3] == tag; q0 += __uint_as_float(t[i][0]); q1 += __uint_as_float(t[i][2]); }
#pragma unroll
      for (int i = 8; i < 12; ++i) { ok &= t[i][1] == tag && t[i][3] == tag; q2 += __uint_as_float(t[i][0]); q3 += __uint_as_float(t[i][2]); }
      if (__all(ok) || spins > SPIN_CAP) break;
      __builtin_amdgcn_s_sleep(2);
    }
    TS(4);
    {
      auto s2 = __builtin_amdgcn_permlane32_swap(__float_as_uint(q2), __float_as_uint(q2), false, false);
      auto s3 = __builtin_amdgcn_permlane32_swap(__float_as_uint(q3), __float_as_uint(q3), false, false);
      q2 = __uint_as_float(s2[0]) + __uint_as_float(s2[1]);
      q3 = __uint_as_float(s3[0]) + __uint_as_float(s3[1]);
    }
    l_q[lane] = pkbf(q0, q1);
    if (lane < 32) l_q[64 + lane] = pkbf(q2, q3);
    if (lane == 0) l_tag[0] = tag;
    asm volatile("s_waitcnt lgkmcnt(0)" ::: "memory");
    if (lane == 0) *(volatile LAS unsigned*)(l_tag + 1) = 1u;
    asm volatile("s_waitcnt lgkmcnt(0)" ::: "memory");
    TS(5);
    TSOUT();
    return;
  }
  const int w = wv - 1;
  const size_t base = ((size_t)h * SK + (SK - W) + s * 512 + w * 64 + kg) * DH + 8 * c;
  const unsigned short* kp = kc + base;
  const unsigned short* vp = vc + base;
  u32x4 kr[24], vr[24];
#define LDK(i) kr[i] = ldnt(kp + ((i) / 3) * 8 * DH + ((i) % 3) * 64)
#define LDV(i) vr[i] = ldnt(vp + ((i) / 3) * 8 * DH + ((i) % 3) * 64)
#pragma unroll
  for (int i = 0; i < WIN; ++i) LDK(i);
  __builtin_amdgcn_sched_barrier(0);
  BAR();
  TS(1);
#pragma unroll
  for (int i = WIN; i < 24 + VPRE; ++i) {
    WAITVM(WIN - 1);
    if (i < 24) LDK(i); else LDV(i - 24);
    __builtin_amdgcn_sched_barrier(0);
  }
  TS(2);
  for (unsigned spins = 0; __builtin_amdgcn_readfirstlane(*(volatile LAS unsigned*)(l_tag + 1)) == 0u && spins < (1u << 22); ++spins) __builtin_amdgcn_s_sleep(1);
  asm volatile("" ::: "memory");
  TS(3);
  u32x4 qr[3];
#pragma unroll
  for (int j = 0; j < 3; ++j) qr[j] = *reinterpret_cast<const u32x4*>(l_q + 32 * j + 4 * c);
  const unsigned tag = l_tag[0];
  constexpr float C2 = 0.07216878364870322f * 1.4426950408889634f;
  float sc[8];
#pragma unroll
  for (int m = 0; m < 8; ++m) {
    float a0 = 0.f, a1 = 0.f;
#pragma unroll
    for (int j = 0; j < 3; ++j) {
      a0 = dot2(kr[m * 3 + j][0], qr[j][0], a0); a1 = dot2(kr[m * 3 + j][1], qr[j][1], a1);
      a0 = dot2(kr[m * 3 + j][2], qr[j][2], a0); a1 = dot2(kr[m * 3 + j][3], qr[j][3], a1);
    }
    sc[m] = sum8(a0 + a1) * C2;
    if (m % 4 == 1 && VPRE + m / 4 < VPRE + VMID) { LDV(VPRE + m / 4); }
    __builtin_amdgcn_sched_barrier(0);
  }
  float mx = sc[0];
#pragma unroll
  for (int m = 1; m < 8; ++m) mx = fmaxf(mx, sc[m]);
  mx = fmaxf(mx, dpp<XOR8>(mx));
  mx = xrow_max(mx);
  float ls = 0.f;
  unsigned pk[8];
#pragma unroll
  for (int m = 0; m < 8; ++m) { const float p = __builtin_amdgcn_exp2f(sc[m] - mx); ls += p; pk[m] = pkbf(p, p); }
  ls += dpp<XOR8>(ls);
  ls = xrow_sum(ls);
  if (lane == 0) { l_ml[w] = mx; l_ml[8 + w] = ls; }
  __builtin_amdgcn_sched_barrier(0);
  TS(4);
  const int c16 = lane & 15, kb = lane >> 4;
  const unsigned short* op = wo + (size_t)(h * DH + 24 * w + kb) * D + 80 * s + 8 * (c16 < 10 ? c16 : 9);
  u32x4 orr[6];
  f32x4 acc[3] = {{0, 0, 0, 0}, {0, 0, 0, 0}, {0, 0, 0, 0}};
  constexpr int V0 = VPRE + VMID;
#pragma unroll
  for (int i = 0; i < 24 + 6; ++i) {
    if (i < 24) {
      const unsigned p = pk[i / 3];
      const u32x4 a = {p & am[0], p & am[1], p & am[2], p & am[3]};
      acc[i % 3] = mfma(a, vr[i], acc[i % 3]);
      __builtin_amdgcn_sched_barrier(0);
    }
    const int n = i + WIN;
    if (n >= V0 && n < 24) { LDV(n); if (n >= WOEARLY && n < WOEARLY + 6) { orr[n - WOEARLY] = ldnt(op + (size_t)(n - WOEARLY) * 4 * D); } }
    __builtin_amdgcn_sched_barrier(0);
  }
  TS(5);
  if ((lane >> 5) == ((lane >> 3) & 1)) {
#pragma unroll
    for (int j = 0; j < 3; ++j) *reinterpret_cast<f32x4*>(l_o + (w * 2 + (lane >> 5)) * 192 + 64 * j + 8 * c + 4 * (kb & 1)) = acc[j];
  }
  BAR();
  TS(6);
  const int t = tid - 64;
  if (t < 128) {
    const f32x4 m0 = *reinterpret_cast<const f32x4*>(l_ml), m1 = *reinterpret_cast<const f32x4*>(l_ml + 4);
    const f32x4 l0 = *reinterpret_cast<const f32x4*>(l_ml + 8), l1 = *reinterpret_cast<const f32x4*>(l_ml + 12);
    const int dw = t >> 4, dkg = (t >> 1) & 7, hf = t & 1;
    const int da = 24 * dw + 4 * (dkg & 1) + (dkg >> 1) + 16 * hf, db = hf ? da : da + 8;
    float va[8], vb[8];
#pragma unroll
    for (int i = 0; i < 8; ++i) { va[i] = l_o[(2 * i) * 192 + da] + l_o[(2 * i + 1) * 192 + da]; vb[i] = l_o[(2 * i) * 192 + db] + l_o[(2 * i + 1) * 192 + db]; }
    const float M = fmaxf(fmaxf(fmaxf(m0[0], m0[1]), fmaxf(m0[2], m0[3])), fmaxf(fmaxf(m1[0], m1[1]), fmaxf(m1[2], m1[3])));
    float L = 0.f, a = 0.f, bb = 0.f;
#pragma unroll
    for (int i = 0; i < 8; ++i) {
      const float wt = __builtin_amdgcn_exp2f((i < 4 ? m0[i & 3] : m1[i & 3]) - M);
      L = fmaf(wt, i < 4 ? l0[i & 3] : l1[i & 3], L); a = fmaf(wt, va[i], a); bb = fmaf(wt, vb[i], bb);
    }
    const float inv = 1.0f / L;
    put2(rs, (int)WS_OP + g * 2048 + t * 16, tag, a * inv, hf ? M + __builtin_amdgcn_logf(L) : bb * inv);
  }
  TS(7);
  u32x4 g0, g1;
  {
    const int off = (int)WS_OP + (h * 8 + c) * 2048 + w * 256 + kg * 32;
    for (unsigned spins = 0;; ++spins) {
      MEMF();
      g0 = get2(rs, off); g1 = get2(rs, off + 16);
      const bool ok = g0[1] == tag && g0[3] == tag && g1[1] == tag && g1[3] == tag;
      if (__all(ok) || spins > SPIN_CAP) break;
      __builtin_amdgcn_s_sleep(PSLEEP);
    }
  }
  TS(8);
  {
    const float lse = __uint_as_float(g1[2]);
    const float M = max8(lse);
    const float wt = __builtin_amdgcn_exp2f(lse - M);
    const float f = wt / sum8(wt);
    f32x4 y = {0, 0, 0, 0};
    const bool odd = c16 >= 8;
    const u32x4 z = {0, 0, 0, 0};
    const u32x4 me = odd ? z : am, mo = odd ? am : z;
#pragma unroll
    for (int u = 0; u < 3; ++u) {
      const float o = sum8(f * __uint_as_float(u == 0 ? g0[0] : u == 1 ? g0[2] : g1[0]));
      const unsigned p = pkbf(o, o);
      const u32x4 ae = {p & me[0], p & me[1], p & me[2], p & me[3]}, ao = {p & mo[0], p & mo[1], p & mo[2], p & mo[3]};
      y = mfma(ae, orr[2 * u], y);
      y = mfma(ao, orr[2 * u + 1], y);
    }
    if (c16 < 10) *reinterpret_cast<f32x4*>(l_y + (w * 2 + (kb >> 1)) * 80 + 8 * c16 + 4 * (kb & 1)) = y;
  }
  BAR();
  TS(9);
  if (t < 40) {
    float a = 0.f, bb = 0.f;
#pragma unroll
    for (int i = 0; i < 16; ++i) { const float2 v = *reinterpret_cast<const float2*>(l_y + i * 80 + 2 * t); a += v.x; bb += v.y; }
    put2(rs, (int)WS_YP + ((s * 10 + (t >> 2)) * 32 + h) * 64 + (t & 3) * 16, tag, a, bb);
  }
  if (w == 1 && h < 30 && (h % 3) == 0) {
    const int cb = h / 3;
    const int off = (int)WS_YP + (s * 10 + cb) * 2048 + lane * 16;
    u32x4 u0, u1;
    for (unsigned spins = 0;; ++spins) {
      MEMF();
      u0 = get2(rs, off); u1 = get2(rs, off + 1024);
      const bool ok = u0[1] == tag && u0[3] == tag && u1[1] == tag && u1[3] == tag;
      if (__all(ok) || spins > SPIN_CAP) break;
      __builtin_amdgcn_s_sleep(PSLEEP);
    }
    TS(10);
    float a = __uint_as_float(u0[0]) + __uint_as_float(u1[0]), bb = __uint_as_float(u0[2]) + __uint_as_float(u1[2]);
    a += dpp<ROR4>(a); bb += dpp<ROR4>(bb);
    a += dpp<XOR8>(a); bb += dpp<XOR8>(bb);
    a = xrow_sum(a); bb = xrow_sum(bb);
    if (lane < 4) reinterpret_cast<unsigned*>(out)[40 * s + 4 * cb + lane] = pkbf(a, bb);
    if (g == 0 && lane == 0) __hip_atomic_store((gu64*)(ws + WS_EPOCH), (u64)tag, RLX_AGENT);
  }
  TS(11);
  TSOUT();
}

extern "C" void kernel_launch(void* const* d_in, const int* in_sizes, int n_in, void* d_out, int out_size, void* d_ws, size_t ws_size, hipStream_t stream) {
  k_fused<<<256, 576, 0, stream>>>((const unsigned short*)d_in[0], (const unsigned short*)d_in[1], (const unsigned short*)d_in[2], (const unsigned short*)d_in[3],
                                   (const unsigned short*)d_in[4], (unsigned short*)d_out, (unsigned char*)d_ws);
}
```

```cpp
#include <hip/hip_runtime.h>
#include <stdint.h>
#include <type_traits>

typedef unsigned u32x4 __attribute__((ext_vector_type(4)));
typedef unsigned u32x16 __attribute__((ext_vector_type(16)));
typedef float f32x2 __attribute__((ext_vector_type(2)));
typedef float f32x4 __attribute__((ext_vector_type(4)));
typedef __bf16 bf16x2 __attribute__((ext_vector_type(2)));
typedef __bf16 bf16x8 __attribute__((ext_vector_type(8)));
typedef unsigned long long u64;
#define GAS __attribute__((address_space(1)))
#define LAS __attribute__((address_space(3)))
#define CAS __attribute__((address_space(4)))
typedef GAS u64 gu64;
#define RLX_AGENT __ATOMIC_RELAXED, __HIP_MEMORY_SCOPE_AGENT

constexpr int D = 640, H = 32, SK = 32768, DH = 192, W = 4096, NQ = H * DH;

constexpr unsigned DB = 1330;
constexpr unsigned DB2 = 1282;
constexpr int DSW = 10;
constexpr unsigned PT0 = 108;
constexpr int SCAP = 24;
constexpr int SE0 = 21, SG0 = 9, HOLD = 147;
constexpr int MA = 4, LB2 = 6, SHB = 8;
constexpr int NSL = 9, P2D = 13;
constexpr int LKB = 4;
constexpr int QKB = 7, QKE = 9;
constexpr int JPB = 9;
constexpr int TVB = 6, TUB = 10, TOB = 12;
constexpr unsigned SPIN_CAP = 1u << 18;

constexpr int WS_EPOCH = 0, WS_START = 64;
constexpr int WS_QP = 4096;
constexpr int WS_OP = WS_QP + 32 * 3072;
constexpr int WS_YP = WS_OP + 256 * 1024;
constexpr int WS_END = WS_YP + 40 * 4096;
constexpr int OOB = 0x40000000;
constexpr int RSRC3 = 0x00020000, AUX_NT = 2, AUX_SC1 = 16;
constexpr int LO_STR = 392, LY_STR = 136;

#define WAITVM(n) asm volatile("s_waitcnt vmcnt(%0)" ::"i"(n) : "memory")
#define WAITLG() asm volatile("s_waitcnt lgkmcnt(0)" ::: "memory")
#define BAR() asm volatile("s_waitcnt lgkmcnt(0)\n\ts_barrier" ::: "memory")
#define MEMF() asm volatile("" ::: "memory")
#define SB() __builtin_amdgcn_sched_barrier(0)
#define PINV(x) asm volatile("" : "+v"(x))
#define PINS(x) asm volatile("" : "+s"(x))

__device__ __forceinline__ float dot2(unsigned k, unsigned q, float acc) {
  return __builtin_amdgcn_fdot2_f32_bf16(__builtin_bit_cast(bf16x2, k), __builtin_bit_cast(bf16x2, q), acc, false);
}
__device__ __forceinline__ f32x4 mfma(u32x4 a, u32x4 b, f32x4 c) {
  return __builtin_amdgcn_mfma_f32_16x16x32_bf16(__builtin_bit_cast(bf16x8, a), __builtin_bit_cast(bf16x8, b), c, 0, 0, 0);
}
__device__ __forceinline__ unsigned pkbf(float lo, float hi) {
  bf16x2 v = {(__bf16)lo, (__bf16)hi};
  return __builtin_bit_cast(unsigned, v);
}
__device__ __forceinline__ float rbf(float x) { return __uint_as_float(pkbf(0.f, x)); }
template <int CTRL> __device__ __forceinline__ float dpp(float x) {
  return __builtin_bit_cast(float, __builtin_amdgcn_mov_dpp(__builtin_bit_cast(int, x), CTRL, 0xf, 0xf, true));
}
constexpr int XOR1 = 0xB1, XOR2 = 0x4E, XOR7 = 0x141, XOR8 = 0x128;
__device__ __forceinline__ float sum8(float x) { x += dpp<XOR1>(x); x += dpp<XOR2>(x); x += dpp<XOR7>(x); return x; }
__device__ __forceinline__ float max8(float x) { x = fmaxf(x, dpp<XOR1>(x)); x = fmaxf(x, dpp<XOR2>(x)); x = fmaxf(x, dpp<XOR7>(x)); return x; }
__device__ __forceinline__ float half_sum(float x) {
  auto t = __builtin_amdgcn_permlane32_swap(__float_as_uint(x), __float_as_uint(x), false, false);
  return __uint_as_float(t[0]) + __uint_as_float(t[1]);
}
__device__ __forceinline__ float xgroup_max(float x) {
  x = fmaxf(x, dpp<XOR8>(x));
  auto s = __builtin_amdgcn_permlane16_swap(__float_as_uint(x), __float_as_uint(x), false, false);
  x = fmaxf(__uint_as_float(s[0]), __uint_as_float(s[1]));
  auto t = __builtin_amdgcn_permlane32_swap(__float_as_uint(x), __float_as_uint(x), false, false);
  return fmaxf(__uint_as_float(t[0]), __uint_as_float(t[1]));
}
template <int N> __device__ __forceinline__ void xgroup_sum(float (&v)[N]) {
#pragma unroll
  for (int u = 0; u < N; ++u) v[u] += dpp<XOR8>(v[u]);
  SB();
  unsigned a[N], b[N];
#pragma unroll
  for (int u = 0; u < N; ++u) { auto s = __builtin_amdgcn_permlane16_swap(__float_as_uint(v[u]), __float_as_uint(v[u]), false, false); a[u] = s[0]; b[u] = s[1]; }
  SB();
#pragma unroll
  for (int u = 0; u < N; ++u) v[u] = __uint_as_float(a[u]) + __uint_as_float(b[u]);
  SB();
#pragma unroll
  for (int u = 0; u < N; ++u) { auto s = __builtin_amdgcn_permlane32_swap(__float_as_uint(v[u]), __float_as_uint(v[u]), false, false); a[u] = s[0]; b[u] = s[1]; }
  SB();
#pragma unroll
  for (int u = 0; u < N; ++u) { v[u] = __uint_as_float(a[u]) + __uint_as_float(b[u]); SB(); }
}
__device__ __forceinline__ __amdgpu_buffer_rsrc_t rsrc(const void* p, int bytes) { return __builtin_amdgcn_make_buffer_rsrc((void*)p, 0, bytes, RSRC3); }
__device__ __forceinline__ u32x4 ldnt(__amdgpu_buffer_rsrc_t r, int voff, int soff) { return __builtin_amdgcn_raw_buffer_load_b128(r, voff, soff, AUX_NT); }
__device__ __forceinline__ void st16(__amdgpu_buffer_rsrc_t r, int off, u32x4 v) { __builtin_amdgcn_raw_buffer_store_b128(v, r, off, 0, AUX_SC1); }
__device__ __forceinline__ u32x4 ld16(__amdgpu_buffer_rsrc_t r, int off) { return __builtin_amdgcn_raw_buffer_load_b128(r, off, 0, AUX_SC1); }
__device__ __forceinline__ void pace(unsigned due) {
  asm volatile("s_branch 2f\n1:\n\ts_sleep 1\n2:\n\ts_memrealtime vcc\n\ts_waitcnt lgkmcnt(0)\n\ts_lshl_b32 vcc_lo, vcc_lo, 4\n\ts_sub_u32 vcc_lo, vcc_lo, %0\n\t"
               "s_cmp_lt_i32 vcc_lo, 0\n\ts_cbranch_scc1 1b" ::"s"(due) : "vcc", "scc", "memory");
}
template <int I, int N, class F> __device__ __forceinline__ void static_for(F&& f) {
  if constexpr (I < N) { f(std::integral_constant<int, I>{}); static_for<I + 1, N>(f); }
}
template <class T> __device__ __forceinline__ T* argptr(const u32x16& a, int i) { return (T*)(((u64)a[2 * i + 1] << 32) | a[2 * i]); }

__global__ void __launch_bounds__(576, 3) __attribute__((aligned(4096))) k_fused(const void*, const void*, const void*, const void*, const void*, void*, void*, u64) {
  __shared__ __attribute__((aligned(16))) float lds[8 * LO_STR + 16 * LY_STR + 96 + 32];
  float* l_o = lds;
  float* l_y = lds + 8 * LO_STR;
  unsigned* l_q = (unsigned*)(l_y + 16 * LY_STR);
  unsigned* l_c = l_q + 96;
  u32x16 ka;
  unsigned b = blockIdx.x;
  asm volatile("s_load_dwordx16 %0, %2, 0x0" : "=&s"(ka), "+s"(b) : "s"(__builtin_amdgcn_kernarg_segment_ptr()));
  SB();
  const unsigned tid = threadIdx.x;
  *(volatile LAS unsigned*)(l_c + (tid & 31)) = 0u;
  SB();
  const unsigned p = b >> 6, sh = (b >> 4) & 3, sl = (b >> 3) & 1;
  const bool two = sh == 3;
  const unsigned prs = two ? D * 2 : 64 * NQ * 2;
  unsigned p0 = (((b & 7) * 4 + p) ^ (two ? 18 : 0)) * (two ? DH * D * 2 : DH * 2) + sl * (two ? 256 : 320 * NQ * 2) + (two ? 896 : sh * 128);
  unsigned p1 = p0 + prs, p2 = p1 + prs, p3 = p2 + prs, p4 = p3 + prs;
  unsigned pro = ((tid >> 3) * (two ? 3 * D * 2 : NQ * 2) + (tid & 7) * 16);
  int w = __builtin_amdgcn_readfirstlane(tid) >> 6;
  PINS(w);
  unsigned pro2 = pro + (two ? D * NQ * 2 : 0);
  PINV(pro); PINV(pro2); PINS(p0); PINS(p1); PINS(p2); PINS(p3); PINS(p4);
  SB();
  asm volatile("s_waitcnt lgkmcnt(0)" : "+s"(ka));
  SB();
  const unsigned short *x = argptr<const unsigned short>(ka, 0), *kc = argptr<const unsigned short>(ka, 1), *vc = argptr<const unsigned short>(ka, 2),
                       *wq = argptr<const unsigned short>(ka, 3), *wo = argptr<const unsigned short>(ka, 4);
  unsigned short* out = argptr<unsigned short>(ka, 5);
  unsigned char* ws = argptr<unsigned char>(ka, 6);
  u64 tanc;
  asm volatile("s_memrealtime %0" : "=s"(tanc));
  SB();
#define ANCHOR() u64 tanc2; asm volatile("s_waitcnt lgkmcnt(0)\n\ts_mov_b64 %0, %1" : "=s"(tanc2) : "s"(tanc)); const unsigned tstart = (unsigned)tanc2
#define IDS() \
  const int s = (b >> 3) & 7, h = (b & 7) * 4 + p, g = h * 8 + s; \
  const int hp = h ^ 18;     \
  const int cb = two ? 2 * s - 6 : s;     \
  const int lane = tid & 63; \
  const int kg = lane >> 3, c = lane & 7, kb = lane >> 4; \
  const __amdgpu_buffer_rsrc_t rs = rsrc(ws, WS_END)
  if (w != 8) {
    const __amdgpu_buffer_rsrc_t rq = rsrc(two ? wo : wq, D * NQ * 2);
    u32x4 wr[5];
    wr[0] = ldnt(rq, pro, p0); SB();
    wr[1] = ldnt(rq, pro, p1); SB();
    wr[2] = ldnt(rq, pro, p2); SB();
    wr[3] = ldnt(rq, pro2, p3); SB();
    wr[4] = ldnt(rq, pro2, p4); SB();
    ANCHOR();
    SB();
    IDS();
    const size_t hb = ((size_t)h * SK + (SK - W)) * DH;
    const __amdgpu_buffer_rsrc_t rk = rsrc(kc + hb, W * DH * 2), rv = rsrc(vc + hb, W * DH * 2);
    const int tvo = (w == 7 && lane < 2) ? lane * (W * DH * 2 - 4096) + s * 256 : OOB;
    unsigned tch = __builtin_amdgcn_raw_buffer_load_b32(rk, tvo, 0, AUX_NT);
    SB();
    u32x4 xs[5];
#pragma unroll
    for (int n = 0; n < 5; ++n) xs[n] = *reinterpret_cast<CAS const u32x4*>((uintptr_t)(x + 320 * sl + 8 * w + 64 * n));
    SB();
    const int kvo = (s * 8 + w) * 3072 + kg * 384 + c * 16;
    u32x4 am;
#pragma unroll
    for (int i = 0; i < 4; ++i) am[i] = (i == (c >> 1)) ? ((c & 1) ? 0xffff0000u : 0x0000ffffu) : 0u;
    auto sel = [&](unsigned v) __attribute__((always_inline)) { return u32x4{v & am[0], v & am[1], v & am[2], v & am[3]}; };
    const bool diag = (lane >> 5) == (kg & 1);
    const bool fin = p == 3 && w == 4 && s < 5;
    const int cg = s * 8 + (b & 7);
    u32x4 kr[24], vr[24], o1[3], qr[3];
    float sc[8], mx, lse;
    unsigned pk[8], xp[5], tag;
    f32x4 acc[3] = {{0, 0, 0, 0}, {0, 0, 0, 0}, {0, 0, 0, 0}};
    asm volatile("" : "+s"(xs[0]), "+s"(xs[1]), "+s"(xs[2]), "+s"(xs[3]), "+s"(xs[4]));
#pragma unroll
    for (int n = 0; n < 5; ++n) {
      const unsigned xx = (kg & 4) ? ((kg & 2) ? xs[n][3] : xs[n][2]) : ((kg & 2) ? xs[n][1] : xs[n][0]);
      xp[n] = __builtin_amdgcn_perm(xx, xx, (kg & 1) ? 0x03020302u : 0x01000100u);
      PINV(xp[n]);
    }
    unsigned due = ((tstart + PT0) << 4) + p * (DB / 4);
    int shs = 0;
    int lat = 0, gro = 0;
    SB();
    asm volatile("s_barrier" ::: "memory");
    SB();
    static_for<0, 16>([&](auto mc) __attribute__((always_inline)) {
      constexpr int m = decltype(mc)::value;
      if constexpr (m > SHB) due -= shs;
      pace(due);
      due += m + 1 >= DSW ? DB2 : DB;
      if constexpr (m >= 1) WAITVM(3 + (m == TVB + 1 || m == TUB + 1 || m == TOB + 1));
      if constexpr (m == 1 || m == TVB + 2 || m == TOB + 2) asm volatile("" ::"v"(tch));
      SB();
#pragma unroll
      for (int j = 0; j < 3; ++j) {
        if constexpr (m < 8) kr[m * 3 + j] = ldnt(rk, kvo + j * 128, m * (64 * 3072));
        else vr[(m - 8) * 3 + j] = ldnt(rv, kvo + j * 128, (m - 8) * (64 * 3072));
        SB();
      }
      if constexpr (m == TVB) tch = __builtin_amdgcn_raw_buffer_load_b32(rv, tvo, 0, AUX_NT);
      if constexpr (m == TOB) tch = __builtin_amdgcn_raw_buffer_load_b32(rsrc(wo + hp * DH * D, DH * D * 2), (w == 7 && lane < 2) ? lane * 191 * D * 2 + cb * 128 : OOB, 0, AUX_NT);
      if constexpr (m == TUB) __builtin_amdgcn_raw_buffer_store_b32(0u, rsrc(out, D * 2), (fin && lane < 8) ? (8 * cg + lane) * 4 : OOB, 0, 0);
      SB();
      if constexpr (m == JPB) asm volatile("s_branch 1f\n\t.p2align 12\n.Lk_fused_page2:\n\ts_branch .Lk_fused_back\n1:");
      SB();
      if constexpr (m == 1) {
        f32x4 aq = {0, 0, 0, 0};
#pragma unroll
        for (int n = 0; n < 5; ++n) aq = mfma(sel(xp[n]), wr[n], aq);
        if (diag) *reinterpret_cast<f32x4*>(l_y + w * 128 + (lane >> 5) * 64 + 8 * c + 4 * (kb & 1)) = aq;
        WAITLG();
        if (lane == 0) *(volatile LAS unsigned*)(l_c + 20 + w) = 1u;
        SB();
      }
      if constexpr (m == QKB) {
        u64 qf;
        for (unsigned spins = 0;; ++spins) {
          qf = *(volatile LAS u64*)(l_c + 16);
          if (__builtin_amdgcn_readfirstlane((unsigned)qf) != 0u || spins > (SPIN_CAP << 4)) break;
          __builtin_amdgcn_s_sleep(1);
        }
        MEMF();
        tag = __builtin_amdgcn_readfirstlane((unsigned)(qf >> 32));
#pragma unroll
        for (int j = 0; j < 3; ++j) qr[j] = *reinterpret_cast<const u32x4*>(l_q + 32 * j + 4 * c);
      }
      if constexpr (m >= MA - 2 && m <= MA) {
        WAITVM(3);
        u64 now;
        asm volatile("s_memrealtime %0\n\ts_waitcnt lgkmcnt(0)" : "=s"(now)::"memory");
        const int e = (int)min((((unsigned)now << 4) - (due - DB)) >> 4, 120u);
        if constexpr (m == MA - 2) gro = 40 - e; else lat += e;
        if constexpr (m == MA) { if (lane == 0) *(volatile LAS unsigned*)(l_c + 20 + w) = (lat >> 1) | (min(max(gro + e, 0), 120) << 10) | (1u << 20); }
        SB();
      }
      if constexpr (m >= QKB && m <= QKE) {
        constexpr int nq = QKE - QKB + 1, k0 = ((m - QKB) * 8 + nq - 1) / nq, k1 = ((m - QKB + 1) * 8 + nq - 1) / nq;
#pragma unroll
        for (int k = k0; k < k1; ++k) {
          float a0 = 0.f, a1 = 0.f;
#pragma unroll
          for (int j = 0; j < 3; ++j) {
            a0 = dot2(kr[k * 3 + j][0], qr[j][0], a0); a1 = dot2(kr[k * 3 + j][1], qr[j][1], a1);
            a0 = dot2(kr[k * 3 + j][2], qr[j][2], a0); a1 = dot2(kr[k * 3 + j][3], qr[j][3], a1);
          }
          sc[k] = rbf(rbf(sum8(a0 + a1)) * 0.07216878364870322f) * 1.4426950408889634f;
          PINV(sc[k]);
          SB();
        }
      }
      if constexpr (m == SHB) {
        unsigned tg;
        for (unsigned spins = 0;; ++spins) {
          tg = __builtin_amdgcn_readfirstlane(*(volatile LAS unsigned*)(l_c + 18));
          if (tg != 0u || spins > (SPIN_CAP << 4)) break;
          __builtin_amdgcn_s_sleep(1);
        }
        const int late = (int)((tstart - tg) << 1) >> 1;
        shs = (late > SCAP ? SCAP : late < -600 ? 0 : late) * 16 / (15 - SHB);
        SB();
      }
      if constexpr (m == QKE) {
        mx = sc[0];
#pragma unroll
        for (int k = 1; k < 8; ++k) mx = fmaxf(mx, sc[k]);
        mx = xgroup_max(mx);
        float ls[1] = {0.f};
#pragma unroll
        for (int k = 0; k < 8; ++k) { sc[k] = __builtin_amdgcn_exp2f(sc[k] - mx); ls[0] += sc[k]; PINV(sc[k]); }
        xgroup_sum<1>(ls);
        if (lane == 0) *(volatile LAS u64*)(l_c + 2 * w) = ((u64)(__float_as_uint(ls[0]) | 1u) << 32) | __float_as_uint(mx);
        SB();
      }
      if constexpr (m == QKE + 1) {
        u64 e;
        for (unsigned spins = 0;; ++spins) {
          e = *(volatile LAS u64*)(l_c + 2 * c);
          if (__all((unsigned)(e >> 32) != 0u) || spins > (SPIN_CAP << 4)) break;
          __builtin_amdgcn_s_sleep(1);
        }
        const float em = __uint_as_float((unsigned)e), el = __uint_as_float((unsigned)(e >> 32));
        const float M = max8(em);
        lse = M + __builtin_amdgcn_logf(sum8(__builtin_amdgcn_exp2f(em - M) * el));
        const float fw = __builtin_amdgcn_exp2f(mx - lse);
#pragma unroll
        for (int k = 0; k < 8; ++k) { const float pv = sc[k] * fw; pk[k] = pkbf(pv, pv); PINV(pk[k]); }
        PINV(lse);
        SB();
      }
      if constexpr (m > QKE) {
#pragma unroll
        for (int k = m == QKE + 1 ? 0 : m - 9; k <= m - 9; ++k) {
          const u32x4 a = sel(pk[k]);
#pragma unroll
          for (int j = 0; j < 3; ++j) { acc[j] = mfma(a, vr[k * 3 + j], acc[j]); PINV(acc[j]); }
          SB();
        }
      }
    });
    const unsigned tg16 = tag << 16;
    unsigned tgn = tg16 | ((__float_as_uint(lse) >> (4 * c)) & 15u);
    LAS float* lw_o = (LAS float*)(l_o + w * LO_STR + (lane >> 5) * 192 + 8 * c + 4 * (kb & 1));
    LAS float* lr_o = (LAS float*)(l_o + kg * LO_STR + 24 * w + 3 * c);
    LAS float* lw_y = (LAS float*)(l_y + (w * 2 + (lane >> 5)) * LY_STR + 8 * c + 4 * (kb & 1));
    LAS float* lr_y = (LAS float*)(l_y + 2 * kg * LY_STR + 16 * w + 2 * c);
    int wol = kg * (3 * D * 2) + c * 16;
    int wos = ((hp * DH + 24 * w) * D + 64 * cb) * 2;
    unsigned kfff0 = 0xfff0u, kperm = 0x05040100u;
    unsigned nsl = p >= 2 ? 0 : NSL;
    PINS(wos); PINS(kfff0); PINS(kperm); PINS(nsl);
    int go_w = lane < 8 ? WS_OP + g * 1024 + w * 128 + lane * 16 : OOB;
    int go_r = WS_OP + (hp * 8 + kg) * 1024 + w * 128 + c * 16;
    int gy_w = (lane < 8 && (two || w < 4)) ? WS_YP + ((cb * 4 + w) * 32 + hp) * 128 + lane * 16 : OOB;
    int gy_r = WS_YP + cg * 4096 + lane * 16;
    int out_w = lane < 8 ? (8 * cg + lane) * 4 : OOB;
    int ep_w = (cg | lane) == 0 ? WS_EPOCH : OOB;
    unsigned cap2 = fin ? SPIN_CAP : 0u;
    PINS(cap2);
    __amdgpu_buffer_rsrc_t ro = rsrc(wo, D * NQ * 2), rout = rsrc(out, D * 2), rsw = rs;
    PINS(ro); PINS(rout);
    u32x4 yv = {0u, tag, 0u, tag};
    int tr = (c * 8 + kg) * 4, c4 = 4 * c;
    u32x4 a7 = sel(pk[7]);
    PINS(rsw); PINV(gy_r); PINV(out_w); PINV(ep_w); PINV(yv); PINV(wol);
    PINV(tgn); PINV(lr_o); PINV(lw_y); PINV(lr_y); PINV(lw_o); PINV(go_w); PINV(go_r); PINV(gy_w); PINV(tr); PINV(c4); PINV(a7);
    SB();
#pragma unroll
    for (int j = 0; j < 3; ++j) { acc[j] = mfma(a7, vr[21 + j], acc[j]); PINV(acc[j]); SB(); }
    if (diag) {
#pragma unroll
      for (int j = 0; j < 3; ++j) *(LAS f32x4*)(lw_o + 64 * j) = acc[j];
    }
    BAR();
    {
      typedef f32x2 __attribute__((aligned(4))) f32x2u;
      f32x2 r0 = *(LAS f32x2u*)(lr_o), r1 = *(LAS f32x2u*)(lr_o + 192), r2 = {lr_o[2], lr_o[194]};
      SB();
      asm volatile("" ::"v"(acc[0]), "v"(acc[1]), "v"(acc[2]));
      SB();
      float v[3];
      v[0] = r0[0] + r1[0]; SB();
      v[1] = r0[1] + r1[1]; SB();
      v[2] = r2[0] + r2[1]; SB();
      xgroup_sum<3>(v);
      const unsigned v1 = __float_as_uint(v[1]);
      st16(rsw, go_w, u32x4{__float_as_uint(v[0]), (v1 & kfff0) | tgn, __float_as_uint(v[2]), (v1 >> 16) | tg16});
    }
    SB();
    u32x4 g0;
    f32x4 y0, y1;
    {
      unsigned t, n = SPIN_CAP;
      asm volatile("s_cmp_eq_u32 %6, 0\n\ts_cbranch_scc0 3f\n\t"
                   "s_sleep 1\n\t"
                   "buffer_load_dwordx4 v[100:103], %7, %8, 0 offen sc1\n"
                   "\ts_barrier\n"
                   "3:\n\tbuffer_load_dwordx4 %2, %10, %11, %12 offen nt\n\tbuffer_load_dwordx4 %3, %10, %11, %12 offen offset:1280 nt\n\t"
                   "buffer_load_dwordx4 %4, %10, %11, %12 offen offset:2560 nt\n\t"
                   "s_cbranch_scc0 4f\n\ts_waitcnt vmcnt(3)\n\ts_branch 5f\n"
                   "4:\n\ts_sleep 3\n\ts_sub_u32 %6, %6, 1\n\ts_cbranch_scc0 4b\n"
                   "1:\n\tbuffer_load_dwordx4 v[100:103], %7, %8, 0 offen sc1\n\ts_waitcnt vmcnt(0)\n"
                   "5:\n\tv_bitop3_b32 %1, v101, v103, %9 bitop3:0x7e\n\tv_cmp_lt_u32_e32 vcc, 0xffff, %1\n\t"
                   "s_cbranch_vccz 2f\n\t"
                   "s_sleep 1\n\ts_sub_u32 %5, %5, 1\n\ts_cbranch_scc0 1b\n"
                   "2:"
                   : "=&{v[100:103]}"(g0), "=&v"(t), "=&v"(o1[0]), "=&v"(o1[1]), "=&v"(o1[2]), "+s"(n), "+s"(nsl)
                   : "v"(go_r), "s"(rsw), "s"(tg16), "v"(wol), "s"(ro), "s"(wos)
                   : "vcc", "scc", "memory");
    }
    SB();
    {
      int nb = (int)((g0[1] & 15u) << c4);
      SB();
      float v[4];
      v[0] = __int_as_float(__builtin_amdgcn_ds_bpermute(tr, (int)g0[0]));
      const unsigned w1 = __builtin_amdgcn_perm(g0[3], g0[1], kperm);
      SB();
      nb |= __builtin_amdgcn_mov_dpp(nb, XOR1, 0xf, 0xf, true);
      SB();
      v[2] = __int_as_float(__builtin_amdgcn_ds_bpermute(tr, (int)g0[2]));
      v[1] = __int_as_float(__builtin_amdgcn_ds_bpermute(tr, (int)(w1 & 0xfffffff0u)));
      SB();
      nb |= __builtin_amdgcn_mov_dpp(nb, XOR2, 0xf, 0xf, true);
      nb |= __builtin_amdgcn_mov_dpp(nb, XOR7, 0xf, 0xf, true);
      float e = __int_as_float(__builtin_amdgcn_ds_bpermute(tr, nb));
      asm volatile("" : "+v"(v[0]), "+v"(v[1]), "+v"(v[2]), "+v"(e));
      SB();
      float M;
      asm volatile("s_nop 1\n\tv_max_f32_dpp %0, %1, %1 quad_perm:[1,0,3,2] row_mask:0xf bank_mask:0xf\n\ts_nop 1\n\t"
                   "v_max_f32_dpp %0, %0, %0 quad_perm:[2,3,0,1] row_mask:0xf bank_mask:0xf\n\ts_nop 1\n\t"
                   "v_max_f32_dpp %0, %0, %0 row_half_mirror row_mask:0xf bank_mask:0xf" : "=&v"(M) : "v"(e));
      v[3] = __builtin_amdgcn_exp2f(e - M);
      SB();
#pragma unroll
      for (int u = 0; u < 3; ++u) { v[u] *= v[3]; PINV(v[u]); }
      SB();
      v[3] += dpp<XOR1>(v[3]); v[0] += dpp<XOR1>(v[0]); v[1] += dpp<XOR1>(v[1]); v[2] += dpp<XOR1>(v[2]);
      SB();
      v[3] += dpp<XOR2>(v[3]); v[0] += dpp<XOR2>(v[0]); v[1] += dpp<XOR2>(v[1]); v[2] += dpp<XOR2>(v[2]);
      SB();
      v[3] += dpp<XOR7>(v[3]);
      SB();
      const float inv = __builtin_amdgcn_rcpf(v[3]);
      v[0] += dpp<XOR7>(v[0]); v[1] += dpp<XOR7>(v[1]); v[2] += dpp<XOR7>(v[2]);
      SB();
      unsigned ob[3];
#pragma unroll
      for (int u = 0; u < 3; ++u) v[u] *= inv;
      SB();
#pragma unroll
      for (int u = 0; u < 3; ++u) ob[u] = pkbf(v[u], v[u]);
      SB();
      y0 = y1 = f32x4{0, 0, 0, 0};
      const u32x4 a0 = sel(ob[0]);
      SB();
      const u32x4 a1 = sel(ob[1]);
      SB();
      y1 = mfma(a0, wr[0], y1);
      const u32x4 a2 = sel(ob[2]);
      SB();
      y1 = mfma(a1, wr[1], y1);
      y1 = mfma(a2, wr[2], y1);
      SB();
      asm volatile("s_waitcnt vmcnt(0)" : "+v"(o1[0]), "+v"(o1[1]), "+v"(o1[2]));
      SB();
      y0 = mfma(a0, o1[0], y0);
      y0 = mfma(a1, o1[1], y0);
      y0 = mfma(a2, o1[2], y0);
      PINV(y0); PINV(y1);
      SB();
      if (diag) { *(LAS f32x4*)(lw_y + 64) = y1; *(LAS f32x4*)(lw_y) = y0; }
    }
    BAR();
    {
      const f32x2 r0 = *(LAS f32x2*)(lr_y), r1 = *(LAS f32x2*)(lr_y + LY_STR);
      SB();
      asm volatile("" ::"v"(y0), "v"(y1));
      SB();
      float v[2] = {r0[0] + r1[0], r0[1] + r1[1]};
      SB();
      xgroup_sum<2>(v);
      yv[0] = __float_as_uint(v[0]); yv[2] = __float_as_uint(v[1]);
      st16(rsw, gy_w, yv);
    }
    SB();
    if (cap2) {
      u32x4 u[4];
      __builtin_amdgcn_s_sleep(P2D);
      {
        unsigned t0, t1, n = cap2;
        asm volatile("1:\n\tbuffer_load_dwordx4 v[100:103], %7, %8, 0 offen sc1\n\tbuffer_load_dwordx4 v[104:107], %7, %8, 0 offen offset:1024 sc1\n\t"
                     "buffer_load_dwordx4 v[108:111], %7, %8, 0 offen offset:2048 sc1\n\tbuffer_load_dwordx4 v[112:115], %7, %8, 0 offen offset:3072 sc1\n\t"
                     "s_waitcnt vmcnt(2)\n\tv_bitop3_b32 %4, v101, v103, %9 bitop3:0x7e\n\tv_bitop3_b32 %5, v105, v107, %9 bitop3:0x7e\n\tv_or_b32_e32 %4, %4, %5\n\t"
                     "s_waitcnt vmcnt(1)\n\tv_bitop3_b32 %5, v109, v111, %9 bitop3:0x7e\n\tv_or_b32_e32 %4, %4, %5\n\t"
                     "s_waitcnt vmcnt(0)\n\tv_bitop3_b32 %5, v113, v115, %9 bitop3:0x7e\n\tv_or_b32_e32 %4, %4, %5\n\t"
                     "v_cmp_ne_u32_e32 vcc, 0, %4\n\t"
                     "s_and_b64 exec, exec, vcc\n\ts_cbranch_scc0 2f\n\t"
                     "s_sleep 1\n\ts_sub_u32 %6, %6, 1\n\ts_cbranch_scc0 1b\n"
                     "2:\n\ts_mov_b64 exec, -1"
                     : "=&{v[100:103]}"(u[0]), "=&{v[104:107]}"(u[1]), "=&{v[108:111]}"(u[2]), "=&{v[112:115]}"(u[3]), "=&v"(t0), "=&v"(t1), "+s"(n)
                     : "v"(gy_r), "s"(rsw), "s"(tag)
                     : "vcc", "scc", "memory");
      }
      SB();
      __builtin_amdgcn_raw_buffer_store_b32(yv[1], rsw, ep_w, 0, AUX_SC1);
      SB();
      float v[2];
      float a1, b1;
      asm volatile("v_add_f32_e32 %0, v100, v104\n\tv_add_f32_e32 %1, v102, v106\n\tv_add_f32_e32 %2, v108, v112\n\tv_add_f32_e32 %3, v110, v114\n\t"
                   "v_add_f32_e32 %0, %0, %2\n\tv_add_f32_e32 %1, %1, %3\n\ts_nop 0"
                   : "=&v"(v[0]), "=&v"(v[1]), "=&v"(a1), "=&v"(b1)
                   : "{v[100:103]}"(u[0]), "{v[104:107]}"(u[1]), "{v[108:111]}"(u[2]), "{v[112:115]}"(u[3]));
      SB();
      xgroup_sum<2>(v);
      __builtin_amdgcn_raw_buffer_store_b32(pkbf(v[0], v[1]), rout, out_w, 0, 0);
    }
    return;
  }
  asm volatile("s_barrier" ::: "memory");
  SB();
  ANCHOR();
  IDS();
  unsigned tag = __hip_atomic_load((GAS unsigned*)(ws + WS_EPOCH), RLX_AGENT);
  asm volatile("s_branch .Lk_fused_page2\n\t.p2align 12\n.Lk_fused_back:");
  tag = __builtin_amdgcn_readfirstlane(tag) + 1u;
  PINS(tag);
  if (!two) {
    for (unsigned spins = 0;; ++spins) {
      const unsigned f = *(volatile LAS unsigned*)(l_c + 20 + c);
      if (__all(f != 0u) || spins > (SPIN_CAP << 2)) break;
      __builtin_amdgcn_s_sleep(2);
    }
    MEMF();
    f32x2 sum = *reinterpret_cast<const f32x2*>(l_y + 2 * lane);
#pragma unroll
    for (int i = 1; i < 8; ++i) sum += *reinterpret_cast<const f32x2*>(l_y + i * 128 + 2 * lane);
    const float a = half_sum(sum[0]), bb = half_sum(sum[1]);
    st16(rs, (WS_QP + h * 3072 + s * 512 + lane * 16) | ((lane & 32) << 25), u32x4{__float_as_uint(a), tag, __float_as_uint(bb), tag});
  }
  if ((b >> 3) == 6) {
    unsigned f;
    for (unsigned spins = 0;; ++spins) {
      f = *(volatile LAS unsigned*)(l_c + 20 + c);
      if (__all(f > 1u) || spins > (SPIN_CAP << 2)) break;
      __builtin_amdgcn_s_sleep(2);
    }
    int sm = (int)f;
    sm += __builtin_amdgcn_mov_dpp(sm, XOR1, 0xf, 0xf, true); sm += __builtin_amdgcn_mov_dpp(sm, XOR2, 0xf, 0xf, true); sm += __builtin_amdgcn_mov_dpp(sm, XOR7, 0xf, 0xf, true);
    if (lane == 0) __hip_atomic_store((gu64*)(ws + WS_START) + (b & 7), ((u64)tag << 32) | (tstart << 20) | ((unsigned)sm & 0xfffffu), RLX_AGENT);
  }
  pace(((tstart + PT0 + 10) << 4) + (4 * LKB + p) * (DB / 4));
  {
    const int off = WS_QP + h * 3072 + lane * 16;
    u32x4 t0, t1, t2;
    for (unsigned spins = 0;; ++spins) {
      MEMF();
      t0 = ld16(rs, off); t1 = ld16(rs, off + 1024); t2 = ld16(rs, off + 2048);
      unsigned bad = (t0[1] ^ tag) | (t0[3] ^ tag), bad2 = (t1[1] ^ tag) | (t1[3] ^ tag), bad3 = (t2[1] ^ tag) | (t2[3] ^ tag);
      PINV(bad); PINV(bad2); PINV(bad3);
      if (!__any((bad | bad2 | bad3) != 0u) || spins > SPIN_CAP) break;
      __builtin_amdgcn_s_sleep(8);
    }
#pragma unroll
    for (int j = 0; j < 3; ++j) {
      const u32x4 t = j == 0 ? t0 : j == 1 ? t1 : t2;
      const float qa = half_sum(__uint_as_float(t[0])), qb = half_sum(__uint_as_float(t[2]));
      l_q[32 * j + (lane & 31)] = pkbf(qa, qb);
    }
  }
  WAITLG();
  if (lane == 0) *(volatile LAS u64*)(l_c + 16) = ((u64)tag << 32) | 1u;
  pace(((tstart + PT0 + 10) << 4) + (4 * LB2 + p) * (DB / 4));
  {
    const u64 e = __hip_atomic_load((gu64*)(ws + WS_START) + c, RLX_AGENT);
    const int d = (int)(((unsigned)e & ~0xfffffu) - (tstart << 20)) >> 20;
    const bool val = (unsigned)(e >> 32) == tag && d > -400 && d < 400;
    int ds = val ? d : 0, dm = ds, dl = val ? (int)(((unsigned)e & 1023u) | (((unsigned)e & 0xffc00u) << 6)) : 400 | (480 << 16);
    ds += __builtin_amdgcn_mov_dpp(ds, XOR1, 0xf, 0xf, true); dm = max(dm, __builtin_amdgcn_mov_dpp(dm, XOR1, 0xf, 0xf, true)); dl += __builtin_amdgcn_mov_dpp(dl, XOR1, 0xf, 0xf, true);
    ds += __builtin_amdgcn_mov_dpp(ds, XOR2, 0xf, 0xf, true); dm = max(dm, __builtin_amdgcn_mov_dpp(dm, XOR2, 0xf, 0xf, true)); dl += __builtin_amdgcn_mov_dpp(dl, XOR2, 0xf, 0xf, true);
    ds += __builtin_amdgcn_mov_dpp(ds, XOR7, 0xf, 0xf, true); dm = max(dm, __builtin_amdgcn_mov_dpp(dm, XOR7, 0xf, 0xf, true)); dl += __builtin_amdgcn_mov_dpp(dl, XOR7, 0xf, 0xf, true);
    const int L = dl & 0xffff, G = (dl >> 16) - 40 * 64;
    const int hold = (min(max(min(L - SE0 * 64, G - SG0 * 64), 0), 256) * HOLD) >> 8;
    if (lane == 0) *(volatile LAS unsigned*)(l_c + 18) = (tstart + (unsigned)(max(ds >> 3, dm - SCAP) + 3 + hold)) | 0x80000000u;
  }
}


extern "C" void kernel_launch(void* const* d_in, const int* in_sizes, int n_in, void* d_out, int out_size, void* d_ws, size_t ws_size, hipStream_t stream) {
  k_fused<<<256, 576, 0, stream>>>(d_in[0], d_in[1], d_in[2], d_in[3], d_in[4], d_out, d_ws, 0ull);
}
```
